```python
import torch

def _softmax(x, axis=-1):
    return torch.softmax(x, dim=axis)
def _gelu(x):
    return torch.nn.functional.gelu(x, approximate="tanh")
def _silu(x):
    return torch.nn.functional.silu(x)
def _sigmoid(x):
    return torch.sigmoid(x)
def _layernorm(x, axis=-1, eps=1e-5):
    m = x.mean(dim=axis, keepdim=True)
    v = x.var(dim=axis, keepdim=True, unbiased=False)
    return (x - m) / torch.sqrt(v + eps)
def _scatter_add(tgt, idx, src, axis=0):
    return tgt.clone().index_add_(axis, idx, src)
def _segment_sum(src, seg, n):
    out = torch.zeros((n,) + src.shape[1:], dtype=src.dtype)
    return out.index_add_(0, seg, src)
def _pad(x, pw):
    flat = tuple(v for pair in reversed(pw) for v in pair)
    return torch.nn.functional.pad(x, flat)

kernel_name = 'standalone__spectral_fnet__prompt'

def setup_inputs():

    _w = (lambda a: (lambda t: t.to(torch.bfloat16) if t.is_floating_point() else t)(torch.as_tensor(a)))
    B, S, D = 1, 2048, 1024
    ns = torch.arange(S)[:, None] * torch.arange(S)[None, :]
    nd = torch.arange(D)[:, None] * torch.arange(D)[None, :]
    Ws_re = torch.cos(-2.0 * torch.pi * ns / S).to(float)
    Ws_im = torch.sin(-2.0 * torch.pi * ns / S).to(float)
    Wd_re = torch.cos(-2.0 * torch.pi * nd / D).to(float)
    Wd_im = torch.sin(-2.0 * torch.pi * nd / D).to(float)
    return {
        "x": _w((lambda *s: torch.randn(*s, dtype=torch.bfloat16) * 0.05)(B, S, D)),
        "dft_seq_re": _w(Ws_re),
        "dft_seq_im": _w(Ws_im),
        "dft_hid_re": _w(Wd_re),
        "dft_hid_im": _w(Wd_im),
    }

def reference(x, dft_seq_re, dft_seq_im, dft_hid_re, dft_hid_im):
    a = torch.einsum("bsd,ed->bse", x, dft_hid_re)
    b = torch.einsum("bsd,ed->bse", x, dft_hid_im)
    y = torch.einsum("ts,bse->bte", dft_seq_re, a) - torch.einsum("ts,bse->bte", dft_seq_im, b)
    return y
```

```cpp
#include <hip/hip_runtime.h>
#include <hip/hip_bf16.h>
#include <stdint.h>

typedef unsigned short u16;
typedef short bf16x8 __attribute__((ext_vector_type(8)));
typedef float f32x16 __attribute__((ext_vector_type(16)));
typedef float f32x2 __attribute__((ext_vector_type(2)));
typedef __bf16 bf16x2_t __attribute__((ext_vector_type(2)));
typedef unsigned u32x4 __attribute__((ext_vector_type(4)));
#define LAS __attribute__((address_space(3)))

constexpr int SEQ = 2048, HID = 1024;
constexpr int NSLOT = 4;
constexpr int SLOT_B = 32768;
constexpr size_t AT_BYTES = 2ull * HID * SEQ * 2;
constexpr size_t XCH_BYTES = 128ull * 2 * 16384;
constexpr unsigned SENT = 0xFFFFFFFFu;

template <int N> __device__ __forceinline__ void wait_vm() { asm volatile("s_waitcnt vmcnt(%0)" ::"n"(N) : "memory"); }
__device__ __forceinline__ void bar_raw() { asm volatile("s_barrier" ::: "memory"); }
__device__ __forceinline__ void bar_lgkm() { asm volatile("s_waitcnt lgkmcnt(0)\n\ts_barrier" ::: "memory"); }

__device__ __forceinline__ unsigned pack_bf16(float lo, float hi) {
    f32x2 v = {lo, hi};
    bf16x2_t r = __builtin_convertvector(v, bf16x2_t);
    return __builtin_bit_cast(unsigned, r);
}
__device__ __forceinline__ int swap23(int r) { return (r & ~12) | ((r & 4) << 1) | ((r & 8) >> 1); }

__device__ __forceinline__ __amdgpu_buffer_rsrc_t make_rsrc(const void* p, unsigned bytes) {
    return __builtin_amdgcn_make_buffer_rsrc((void*)p, 0, bytes, 0x00020000);
}

template <int NK>
__device__ __forceinline__ void loader(LAS char* smem, int lw, __amdgpu_buffer_rsrc_t rsrc, unsigned voffE, unsigned voffO,
                                       unsigned piece_stride, unsigned step_stride) {
    auto issue = [&](int stage, int slot) {
        unsigned so = (unsigned)stage * step_stride;
#pragma unroll
        for (int i = 0; i < 8; ++i) {
            __builtin_amdgcn_raw_ptr_buffer_load_lds(rsrc, (LAS void*)(smem + slot * SLOT_B + (lw * 8 + i) * 1024), 16,
                                                     (i & 1) ? voffO : voffE, so + i * piece_stride, 0, 0);
        }
    };
#pragma unroll
    for (int s = 0; s < NSLOT; ++s) issue(s, s);
    wait_vm<(NSLOT - 1) * 8>();
    bar_raw();
    wait_vm<(NSLOT - 2) * 8>();
    bar_raw();
    int slot = 0;
    for (int k = 0; k < NK - NSLOT; ++k) {
        issue(k + NSLOT, slot);
        slot = (slot + 1 == NSLOT) ? 0 : slot + 1;
        wait_vm<(NSLOT - 2) * 8>();
        bar_raw();
    }
#pragma unroll
    for (int r = NSLOT - 3; r >= 0; --r) {
        if (r == 2) wait_vm<16>();
        if (r == 1) wait_vm<8>();
        if (r == 0) wait_vm<0>();
        bar_raw();
    }
}

template <int NK, int STRIDE_A, int STRIDE_B>
__device__ __forceinline__ void consumer(LAS char* smem, const unsigned (&offA)[4], const unsigned (&offB)[4],
                                         f32x16 (&acc)[2][2]) {
    bf16x8 fa[2][4][2], fb[2][4][2];
    auto rd = [&](int set, int slot) {
#pragma unroll
        for (int kk = 0; kk < 4; ++kk) {
#pragma unroll
            for (int i = 0; i < 2; ++i) {
                fa[set][kk][i] = *(const LAS bf16x8*)(smem + slot * SLOT_B + offA[kk] + i * STRIDE_A);
                fb[set][kk][i] = *(const LAS bf16x8*)(smem + slot * SLOT_B + offB[kk] + i * STRIDE_B);
            }
        }
    };
    auto mm = [&](int set) {
#pragma unroll
        for (int kk = 0; kk < 4; ++kk)
#pragma unroll
            for (int i = 0; i < 2; ++i)
#pragma unroll
                for (int j = 0; j < 2; ++j)
                    acc[i][j] = __builtin_amdgcn_mfma_f32_32x32x16_bf16(fa[set][kk][i], fb[set][kk][j], acc[i][j], 0, 0, 0);
    };
    bar_raw();
    rd(0, 0);
    bar_lgkm();
    static_assert(NSLOT == 4, "loop below is unrolled for 4 slots");
    for (int k = 0; k < NK; k += 4) {
#pragma unroll
        for (int u = 0; u < 4; ++u) {
            rd((u + 1) & 1, (u + 1) & 3);
            mm(u & 1);
#pragma unroll
            for (int g = 0; g < 8; ++g) {
                __builtin_amdgcn_sched_group_barrier(0x008, 1, 0);
                __builtin_amdgcn_sched_group_barrier(0x100, 2, 0);
            }
            __builtin_amdgcn_sched_group_barrier(0x008, 8, 0);
            __builtin_amdgcn_sched_barrier(0);
            bar_lgkm();
            __builtin_amdgcn_sched_barrier(0);
        }
    }
}

__global__ __launch_bounds__(512, 2) void fnet_stage1(const u16* __restrict__ x, const u16* __restrict__ wre,
                                                      const u16* __restrict__ wim, u16* __restrict__ at,
                                                      unsigned* xch) {
    extern __shared__ __attribute__((aligned(16))) char smem_g[];
    LAS char* smem = (LAS char*)smem_g;
    const int b = blockIdx.x, xcd = b & 7, j = b >> 3;
    const int ts = (xcd & 3) * 4 + (j & 3);
    const int z = xcd >> 2, te = j >> 2;
    const int wid = __builtin_amdgcn_readfirstlane(threadIdx.x >> 6), lane = threadIdx.x & 63;
    constexpr int NK = HID / 64;
    if (wid >= 4) {
        const int lw = wid - 4;
        const u16* base = (lw < 2) ? x + (size_t)ts * 128 * HID : (z ? wim : wre) + (size_t)te * 128 * HID;
        const int r = lane >> 3, c = lane & 7;
        const unsigned row0 = (lw & 1) * 64 + r;
        const unsigned voffE = row0 * (HID * 2) + ((c ^ (r >> 1)) << 4);
        const unsigned voffO = row0 * (HID * 2) + ((c ^ (4 + (r >> 1))) << 4);
        loader<NK>(smem, lw, make_rsrc(base, 128 * HID * 2), voffE, voffO, 8 * HID * 2, 128);
        return;
    }
    {
        u32x4 s4 = {SENT, SENT, SENT, SENT};
        u32x4* p = (u32x4*)(xch) + (size_t)b * 1024 + threadIdx.x;
#pragma unroll
        for (int i = 0; i < 4; ++i) p[i * 256] = s4;
    }
    const int wm = wid >> 1, wn = wid & 1;
    const int h = lane >> 5, rr = swap23(lane & 31);
    unsigned offA[4], offB[4];
#pragma unroll
    for (int kk = 0; kk < 4; ++kk) {
        const unsigned sw = (((2 * kk + h) ^ ((rr >> 1) & 7)) << 4);
        offA[kk] = (wm * 64 + rr) * 128 + sw;
        offB[kk] = 16384 + (wn * 64 + rr) * 128 + sw;
    }
    f32x16 acc[2][2] = {};
    consumer<NK, 4096, 4096>(smem, offA, offB, acc);
    u32x4* dst = (u32x4*)at + ((size_t)((z * 8 + te) * 32 + ts * 2 + wm) * 16) * 64 + lane;
#pragma unroll
    for (int i = 0; i < 2; ++i)
#pragma unroll
        for (int jj = 0; jj < 2; ++jj)
#pragma unroll
            for (int u = 0; u < 2; ++u) {
                u32x4 v;
#pragma unroll
                for (int q = 0; q < 4; ++q) v[q] = pack_bf16(acc[i][jj][8 * u + 2 * q], acc[i][jj][8 * u + 2 * q + 1]);
                dst[((wn * 2 + jj) * 4 + i * 2 + u) * 64] = v;
            }
}

__global__ __launch_bounds__(512, 2) void fnet_stage2(const u16* __restrict__ sre, const u16* __restrict__ sim,
                                                      const u16* __restrict__ at, unsigned* xch,
                                                      u16* __restrict__ y) {
    extern __shared__ __attribute__((aligned(16))) char smem_g[];
    LAS char* smem = (LAS char*)smem_g;
    const int b = blockIdx.x, xcd = b & 7, j = b >> 3;
    const int tt = (xcd & 3) * 4 + (j & 3);
    const int z = xcd >> 2, te = j >> 2;
    const int wid = __builtin_amdgcn_readfirstlane(threadIdx.x >> 6), lane = threadIdx.x & 63;
    constexpr int NK = SEQ / 64;
    if (wid >= 4) {
        const int lw = wid - 4;
        if (lw < 2) {
            const u16* base = at + (size_t)(z * 8 + te) * 32 * 8192;
            const unsigned voff = lw * 8192 + lane * 16;
            loader<NK>(smem, lw, make_rsrc(base, 32 * 16384), voff, voff, 1024, 16384);
        } else {
            const u16* base = (z ? sim : sre) + (size_t)tt * 128 * SEQ;
            const int r = lane >> 3, c = lane & 7;
            const unsigned row0 = (lw & 1) * 64 + r;
            const unsigned voffE = row0 * (SEQ * 2) + ((c ^ (r >> 1)) << 4);
            const unsigned voffO = row0 * (SEQ * 2) + ((c ^ (4 + (r >> 1))) << 4);
            loader<NK>(smem, lw, make_rsrc(base, 128 * SEQ * 2), voffE, voffO, 8 * SEQ * 2, 128);
        }
        return;
    }
    const int wm = wid >> 1, wn = wid & 1;
    const int h = lane >> 5, rr = lane & 31;
    unsigned offA[4], offB[4];
#pragma unroll
    for (int kk = 0; kk < 4; ++kk) {
        const unsigned sw = (((2 * kk + h) ^ ((rr >> 1) & 7)) << 4);
        offA[kk] = (wm * 8 + kk) * 1024 + lane * 16;
        offB[kk] = 16384 + (wn * 64 + rr) * 128 + sw;
    }
    f32x16 acc[2][2] = {};
    consumer<NK, 4096, 4096>(smem, offA, offB, acc);
    const int tile = tt * 8 + te;
    u32x4 own[8];
#pragma unroll
    for (int i = 0; i < 2; ++i)
#pragma unroll
        for (int jj = 0; jj < 2; ++jj)
#pragma unroll
            for (int u = 0; u < 2; ++u)
#pragma unroll
                for (int q = 0; q < 4; ++q)
                    own[(jj * 2 + i) * 2 + u][q] = pack_bf16(acc[i][jj][8 * u + 2 * q], acc[i][jj][8 * u + 2 * q + 1]);
    const __amdgpu_buffer_rsrc_t xr = make_rsrc(xch, (unsigned)XCH_BYTES);
    const unsigned box = (unsigned)(((tile * 2 + wn) * 2 + wm) * 8192) + lane * 16;
    if (wn != z) {
#pragma unroll
        for (int c = 0; c < 8; ++c) {
            u32x4 v = own[c];
#pragma unroll
            for (int q = 0; q < 4; ++q) v[q] = v[q] < (SENT - 1u) ? v[q] : (SENT - 1u);
            __builtin_amdgcn_raw_buffer_store_b128(v, xr, box + c * 1024, 0, 16);
        }
        return;
    }
    u32x4 oth[8];
    for (unsigned spin = 0; spin < (1u << 18); ++spin) {
        unsigned mx = 0;
        asm volatile("" ::: "memory");
#pragma unroll
        for (int c = 0; c < 8; ++c) oth[c] = __builtin_amdgcn_raw_buffer_load_b128(xr, box + c * 1024, 0, 16);
#pragma unroll
        for (int c = 0; c < 8; ++c)
#pragma unroll
            for (int q = 0; q < 4; ++q) mx = mx > oth[c][q] ? mx : oth[c][q];
        if (__all(mx != SENT)) break;
        __builtin_amdgcn_s_sleep(2);
    }
    const float sg = z ? -1.0f : 1.0f;
    u16* yrow = y + (size_t)(tt * 128 + wn * 64 + rr) * HID + te * 128 + wm * 64 + h * 8;
#pragma unroll
    for (int jj = 0; jj < 2; ++jj)
#pragma unroll
        for (int i = 0; i < 2; ++i)
#pragma unroll
            for (int u = 0; u < 2; ++u) {
                const int c = (jj * 2 + i) * 2 + u;
                u32x4 o;
#pragma unroll
                for (int q = 0; q < 4; ++q) {
                    const float olo = __builtin_bit_cast(float, own[c][q] << 16), ohi = __builtin_bit_cast(float, own[c][q] & 0xFFFF0000u);
                    const float plo = __builtin_bit_cast(float, oth[c][q] << 16), phi = __builtin_bit_cast(float, oth[c][q] & 0xFFFF0000u);
                    o[q] = pack_bf16((olo - plo) * sg, (ohi - phi) * sg);
                }
                *(u32x4*)(yrow + (size_t)jj * 32 * HID + i * 32 + u * 16) = o;
            }
}

extern "C" void kernel_launch(void* const* d_in, const int* in_sizes, int n_in, void* d_out, int out_size, void* d_ws,
                              size_t ws_size, hipStream_t stream) {
    const u16* x = (const u16*)d_in[0];
    const u16* sre = (const u16*)d_in[1];
    const u16* sim = (const u16*)d_in[2];
    const u16* wre = (const u16*)d_in[3];
    const u16* wim = (const u16*)d_in[4];
    u16* at = (u16*)d_ws;
    unsigned* xch = (unsigned*)((char*)d_ws + AT_BYTES);
    constexpr int SHM = NSLOT * SLOT_B;
    (void)hipFuncSetAttribute((const void*)fnet_stage1, hipFuncAttributeMaxDynamicSharedMemorySize, SHM);
    (void)hipFuncSetAttribute((const void*)fnet_stage2, hipFuncAttributeMaxDynamicSharedMemorySize, SHM);
    fnet_stage1<<<256, 512, SHM, stream>>>(x, wre, wim, at, xch);
    fnet_stage2<<<256, 512, SHM, stream>>>(sre, sim, at, xch, (u16*)d_out);
}
```

```cpp
#include <hip/hip_runtime.h>
#include <hip/hip_bf16.h>
#include <stdint.h>

typedef unsigned short u16;
typedef unsigned long long u64;
typedef short bf16x8 __attribute__((ext_vector_type(8)));
typedef float f32x4 __attribute__((ext_vector_type(4)));
typedef float f32x2 __attribute__((ext_vector_type(2)));
typedef __bf16 bf16x2_t __attribute__((ext_vector_type(2)));
typedef unsigned u32x4 __attribute__((ext_vector_type(4)));
#define LAS __attribute__((address_space(3)))
#define GAS __attribute__((address_space(1)))

constexpr int SEQ = 2048, HID = 1024;
constexpr int UNIT = 16384;
constexpr int XR_OFF = 6 * UNIT;
constexpr int SHM = 10 * UNIT;
constexpr size_t AT_BYTES = 2ull * HID * SEQ * 2;
constexpr int CTL_U64 = 128;
constexpr unsigned SENT = 0xFFFFFFFFu;
constexpr unsigned SPIN_CAP = 1u << 18;
constexpr int SC1 = 16;

constexpr int K1 = 6;
constexpr int QB = 16 + K1;
constexpr int NA = QB / 2;
constexpr int NW = 26 - K1;
constexpr int qhalf(int q) { return q < 2 * K1 ? (q & 1) : (q >= QB); }
constexpr int qfill(int q) { return q < 2 * K1 ? (q >> 1) : (q < QB ? q - K1 : q - QB + 10); }
constexpr int fillk(int f) { return f < 16 ? f : f - 16 + K1; }
constexpr int qk(int q) { return fillk(qfill(q)); }
constexpr int regA(int i) { return (i & 3) == 0 ? 6 : qfill((i & 3) == 1 ? 29 : (i & 3) == 2 ? 28 : 30) % 6; }
constexpr int regB(int i) { return (i & 3) < 2 ? qfill(26 + (i & 3)) % 6 : 5 + (i & 3); }
constexpr int firstq(int f) { for (int q = 0; q < 32; ++q) if (qfill(q) == f) return q; return 0; }
constexpr int lastq(int f) { for (int q = 31; q >= 0; --q) if (qfill(q) == f && (q >= 2 * K1 || !(q & 1))) return q; return 0; }
constexpr int bdead(int f) { return f < NW ? firstq(f) / 2 - 2 : 14 + (f - NW) - (f - NW >= 30); }
constexpr int bearly(int f) {
    return f < 6 ? -2 : f < NW ? lastq(f - 6) / 2 : f - NW < 3 ? 13 : f - NW == 3 ? 14 : 12 + (f - NW);
}
struct BSched {
    int issue[64], dead[64], allow[64];
    bool ok;
    constexpr BSched() : issue{}, dead{}, allow{}, ok(true) {
        int p1 = -9, p2 = -9;
        for (int g = 0; g < NW + 32; ++g) {
            const int e = bearly(g);
            dead[g] = bdead(g);
            int v = g < NW ? dead[g] - 3 : e;
            if (v < e) v = e;
            if (v < g / 2 - 2) v = g / 2 - 2;
            if (v < p1) v = p1;
            if (v == p2) ++v;
            p2 = p1; p1 = v;
            issue[g] = v;
            if (v > dead[g] || (g && dead[g] < dead[g - 1])) ok = false;
        }
        for (int n = -2; n < 46; ++n) {
            int c = 0;
            for (int f = 0; f < NW + 32; ++f) if (issue[f] <= n && dead[f] > n) ++c;
            allow[n + 2] = c;
            if (c > 5) ok = false;
        }
    }
};
constexpr BSched kB{};
static_assert(kB.ok, "loader schedule");
constexpr int FILL0 = 2;
constexpr int DRA = NA, DRB = 21;
constexpr int CKA = 14, CKA2 = 16, CKA3 = 20, CKB = 28;
static_assert(DRA <= 12, "own lo slice is loaded in interval 13");
constexpr int wfind(int n) {
    constexpr int d[16] = {9, 10, 12, 15, 17, 18, 21, 23, 25, 27, 29, 31, 33, 35, 37, 39};
    for (int t = 0; t < 16; ++t) if (d[t] == n) return t;
    return -1;
}
__device__ __forceinline__ int kappa(int i, int tm) {
    return i == 0 ? tm : i == 1 ? 16 + tm : i <= 16 ? (tm ^ (i - 1)) : 16 + (tm ^ (i - 16));
}
template <int I> struct IC { static constexpr int v = I; };
template <int I, int N, class F> __device__ __forceinline__ void static_for(F&& f) {
    if constexpr (I < N) { f(IC<I>{}); static_for<I + 1, N>(f); }
}

template <int N> __device__ __forceinline__ void wait_vm() { asm volatile("s_waitcnt vmcnt(%0)" ::"n"(N) : "memory"); }
__device__ __forceinline__ void bar_raw() { asm volatile("s_barrier" ::: "memory"); }
__device__ __forceinline__ void bar_lgkm() {
    __builtin_amdgcn_sched_barrier(0);
    __builtin_amdgcn_s_waitcnt(0xC07F);
    asm volatile("s_barrier" ::: "memory");
    __builtin_amdgcn_sched_barrier(0);
}
__device__ __forceinline__ void cfence() { asm volatile("" ::: "memory"); }
__device__ __forceinline__ unsigned pack_bf16(float lo, float hi) {
    f32x2 v = {lo, hi};
    bf16x2_t r = __builtin_convertvector(v, bf16x2_t);
    return __builtin_bit_cast(unsigned, r);
}
__device__ __forceinline__ u32x4 pack8(f32x4 a, f32x4 b) {
    u32x4 r = {pack_bf16(a[0], a[1]), pack_bf16(a[2], a[3]), pack_bf16(b[0], b[1]), pack_bf16(b[2], b[3])};
    return r;
}
__device__ __forceinline__ __amdgpu_buffer_rsrc_t make_rsrc(const void* p, unsigned bytes) {
    return __builtin_amdgcn_make_buffer_rsrc((void*)p, 0, bytes, 0x00020000);
}
__device__ __forceinline__ void touch64(unsigned& sink, const void* p) {
    asm volatile("global_load_dword %0, %1, off" : "+v"(sink) : "v"(p) : "memory");
}
__device__ __forceinline__ void interleave(int reads) {
#pragma unroll
    for (int g = 0; g < 16; ++g) {
        if (g < reads) {
            __builtin_amdgcn_sched_group_barrier(0x008, 1, 0);
            __builtin_amdgcn_sched_group_barrier(0x100, 1, 0);
        }
    }
    __builtin_amdgcn_sched_group_barrier(0x008, 16, 0);
    __builtin_amdgcn_sched_barrier(0);
}

template <bool PERM>
__device__ __forceinline__ void row_offsets(unsigned (&vo)[8], int lane, unsigned ldb, int rx) {
    const int r = lane >> 3, c = lane & 7;
#pragma unroll
    for (int i = 0; i < 8; ++i) {
        const int row = PERM ? 32 * (i >> 2) + 16 * (i & 1) + 4 * ((i >> 1) & 1) + 8 * (r >> 2) + (r & 3) : (8 * i + r) ^ rx;
        vo[i] = row * ldb + ((c ^ (4 * (i & 1) + (r >> 1))) << 4) - (i & 3) * 1024;
        asm volatile("" : "+v"(vo[i]));
    }
}
__device__ __forceinline__ void issue_rows(LAS char* dst, __amdgpu_buffer_rsrc_t rsrc, const unsigned (&vo)[8], unsigned so) {
#pragma unroll
    for (int h = 0; h < 2; ++h) {
        __builtin_amdgcn_raw_ptr_buffer_load_lds(rsrc, (LAS void*)(dst + h * 4096), 16, vo[4 * h + 0], so, 0, 0);
        __builtin_amdgcn_raw_ptr_buffer_load_lds(rsrc, (LAS void*)(dst + h * 4096), 16, vo[4 * h + 1], so, 1024, 0);
        __builtin_amdgcn_raw_ptr_buffer_load_lds(rsrc, (LAS void*)(dst + h * 4096), 16, vo[4 * h + 2], so, 2048, 0);
        __builtin_amdgcn_raw_ptr_buffer_load_lds(rsrc, (LAS void*)(dst + h * 4096), 16, vo[4 * h + 3], so, 3072, 0);
    }
}
__device__ __forceinline__ void issue_at(LAS char* dst, __amdgpu_buffer_rsrc_t rsrc, const unsigned (&vo)[4], unsigned so) {
#pragma unroll
    for (int i = 0; i < 4; ++i) {
        __builtin_amdgcn_raw_ptr_buffer_load_lds(rsrc, (LAS void*)(dst + i * 4096), 16, vo[i], so, 0, SC1);
        __builtin_amdgcn_raw_ptr_buffer_load_lds(rsrc, (LAS void*)(dst + i * 4096), 16, vo[i], so, 1024, SC1);
        __builtin_amdgcn_raw_ptr_buffer_load_lds(rsrc, (LAS void*)(dst + i * 4096), 16, vo[i], so, 2048, SC1);
        __builtin_amdgcn_raw_ptr_buffer_load_lds(rsrc, (LAS void*)(dst + i * 4096), 16, vo[i], so, 3072, SC1);
    }
}

__global__ __launch_bounds__(512, 2) void fnet_fused(const u16* __restrict__ x, const u16* __restrict__ sre,
                                                     const u16* __restrict__ sim, const u16* __restrict__ wre,
                                                     const u16* __restrict__ wim, u16* at, u64* ctl, u16* y) {
    extern __shared__ __attribute__((aligned(16))) char smem_g[];
    LAS char* smem = (LAS char*)smem_g;
    const int b = blockIdx.x, xcd = b & 7, j = b >> 3;
    const int tm = (xcd & 3) * 4 + (j & 3);
    const int z = xcd >> 2, te = j >> 2;
    const int wid = __builtin_amdgcn_readfirstlane(threadIdx.x >> 6), lane = threadIdx.x & 63;
    const int k0 = 4 * (xcd & 3) + 8 * z;
    unsigned sink = 0;
    if (wid < 4) {
        const u16* src = wid < 2 ? (z ? wim : wre) + (size_t)(32 * j + 16 * wid + (lane >> 2)) * HID + ((k0 + (lane & 3)) & 15) * 64
                                 : x + (size_t)((wid & 1) * 1024 + (xcd & 3) * 256 + 8 * j + (lane >> 3)) * HID + ((k0 + (lane & 7)) & 15) * 64;
        touch64(sink, src);
    }
    if (wid == 0 && j < 4) {
        unsigned long long pc0;
        asm volatile("s_getpc_b64 %0" : "=s"(pc0));
        touch64(sink, (const char*)pc0 + 4096 + j * 8192 + lane * 128);
    }
    const u16* ws = (z ? sim : sre) + (size_t)tm * 128 * SEQ;

    if (wid == 5) {
        GAS u64* grp = (GAS u64*)ctl + te * CTL_U64;
        {
            const char* p = lane < 8    ? (const char*)ws + lane * 65536
                            : lane < 12 ? (const char*)(y + (size_t)(tm * 128 + z * 32 + ((lane & 1) * 64)) * HID + te * 128)
                                        : (const char*)(at + (size_t)((z * 8 + te) * 32 + tm + (lane & 1) * 16) * 8192);
            asm volatile("global_load_dword %0, %1, off sc1" : "+v"(sink) : "v"(p) : "memory");
        }
        u64 tag = __hip_atomic_load(grp + 64, __ATOMIC_RELAXED, __HIP_MEMORY_SCOPE_AGENT) + 1;
        const char* pw = (const char*)(ws + (size_t)(lane >> 1) * SEQ);
        const char* pw0 = pw + kappa(2 * te + (lane & 1), tm) * 128;
        const char* pw1 = pw + kappa(16 + 2 * te + (lane & 1), tm) * 128;
        const __amdgpu_buffer_rsrc_t br = make_rsrc(y + (size_t)(tm * 128 + z * 32) * HID + te * 128, 96 * HID * 2);
        const unsigned bo = (unsigned)(lane >> 4) * (HID * 2) + (lane & 15) * 16;
        const u32x4 s4 = {SENT, SENT, SENT, SENT};
        bar_raw();
        bar_raw();
        asm volatile("" : "+v"(tag));
        u64 pv = 0;
        static_for<0, 45>([&](auto N) {
            constexpr int n = decltype(N)::v, rnd = n > 15, t = wfind(n);
            if constexpr (n == DRA + 1 || n == DRB + 1) {
                if constexpr (rnd) wait_vm<0>();
                if (lane == 0)
                    __hip_atomic_store(grp + rnd * 32 + z * 16 + tm, tag, __ATOMIC_RELAXED, __HIP_MEMORY_SCOPE_AGENT);
            }
            if constexpr (n >= FILL0 && n < FILL0 + 8) {
                constexpr int row = ((n - FILL0) >> 2) * 64 + ((n - FILL0) & 3) * 8;
                __builtin_amdgcn_raw_buffer_store_b128(s4, br, bo, row * HID * 2, SC1);
                __builtin_amdgcn_raw_buffer_store_b128(s4, br, bo, (row + 4) * HID * 2, SC1);
            }
            if constexpr (t >= 0) {
                if (__builtin_expect((t & 7) == te, 0)) {
                    const char* p = t >= 8 ? pw1 : pw0;
#pragma unroll
                    for (int i = 0; i < 4; ++i) touch64(sink, p + (size_t)i * 32 * SEQ * 2);
                }
            }
            constexpr bool hi = n > CKA3;
            constexpr int lim = n <= CKA ? 4 : n <= CKA2 ? 8 : 16;
            GAS u64* f = grp + (hi ? 32 + (lane & 31) : z * 16 + (tm ^ (lane & (lim - 1))));
            if constexpr (n == CKA - 1 || n == CKA2 - 1 || n == CKA3 - 1 || n == CKB - 2)
                pv = __hip_atomic_load(f, __ATOMIC_RELAXED, __HIP_MEMORY_SCOPE_AGENT);
            if constexpr (n == CKA || n == CKA2 || n == CKA3 || n == CKB) {
                if (__builtin_expect(!__all(pv == tag), 0)) {
#pragma unroll 1
                    for (unsigned spin = 0; spin < SPIN_CAP; ++spin) {
                        const u64 v = __hip_atomic_load(f, __ATOMIC_RELAXED, __HIP_MEMORY_SCOPE_AGENT);
                        if (__all(v == tag)) break;
                        __builtin_amdgcn_s_sleep(1);
                    }
                }
                if (hi && b == te * 32 && lane == 0)
                    __hip_atomic_store(grp + 64, tag, __ATOMIC_RELAXED, __HIP_MEMORY_SCOPE_AGENT);
            }
            bar_raw();
        });
        asm volatile("s_waitcnt vmcnt(0)" : "+v"(sink)::"memory");
        return;
    }
    if (wid >= 4) {
        unsigned vo[8];
        row_offsets<true>(vo, lane, HID * 2, 0);
        if (wid == 4) {
            const __amdgpu_buffer_rsrc_t r0 = make_rsrc(x + (size_t)tm * 64 * HID, (1024 + 64) * HID * 2);
            const __amdgpu_buffer_rsrc_t r1 = make_rsrc(at + (size_t)(z * 8 + te) * 32 * 8192, 32 * 16384);
            unsigned va[4];
#pragma unroll
            for (int i = 0; i < 4; ++i) { va[i] = lane * 16 + i * 4096; asm volatile("" : "+v"(va[i])); }
            auto ldx = [&](int q) {
                issue_rows(smem + XR_OFF + (q & 7) * 8192, r0, vo, qhalf(q) * 1024 * HID * 2 + ((qk(q) + k0) & 15) * 128);
            };
            auto lda = [&](int i) { issue_at(smem + regA(i) * UNIT, r1, va, kappa(i, tm) * 16384); };
#pragma unroll
            for (int q = 0; q < 4; ++q) ldx(q);
            wait_vm<16>(); bar_raw();
#pragma unroll
            for (int q = 4; q < 8; ++q) ldx(q);
            wait_vm<32>(); bar_raw();
#pragma unroll
            for (int n = 0; n < 12; ++n) { ldx(2 * n + 8); ldx(2 * n + 9); wait_vm<32>(); bar_raw(); }
            wait_vm<16>(); bar_raw();
            lda(0); wait_vm<16>(); bar_raw();
            wait_vm<0>(); bar_raw();
            lda(2); lda(3); bar_raw();
            lda(4); wait_vm<32>(); bar_raw();
#pragma unroll
            for (int i = 5; i < 31; ++i) { lda(i); wait_vm<32>(); bar_raw(); }
            lda(31); wait_vm<16>(); bar_raw();
            wait_vm<0>(); bar_raw();
        } else {
            const int hf = wid & 1;
            const __amdgpu_buffer_rsrc_t r0 = make_rsrc((z ? wim : wre) + (size_t)(te * 128 + hf * 64) * HID, 64 * HID * 2);
            const __amdgpu_buffer_rsrc_t r1 = make_rsrc(ws + (size_t)hf * 64 * SEQ, 64 * SEQ * 2);
            unsigned vw[8];
            row_offsets<false>(vw, lane, SEQ * 2, z * 32);
            LAS char* dst = smem + hf * 8192;
            static_for<0, 47>([&](auto N) {
                constexpr int n = decltype(N)::v - 2;
                static_for<0, NW + 32>([&](auto F) {
                    constexpr int f = decltype(F)::v;
                    if constexpr (kB.issue[f] == n) {
                        if constexpr (f < NW) issue_rows(dst + (f % 6) * UNIT, r0, vo, ((fillk(f) + k0) & 15) * 128);
                        else issue_rows(dst + regB(f - NW) * UNIT, r1, vw, kappa(f - NW, tm) * 128);
                    }
                });
                wait_vm<8 * kB.allow[n + 2]>();
                bar_raw();
            });
        }
        return;
    }

    const int wm = wid >> 1, wn = wid & 1;
    const int q4 = lane >> 4, rr = lane & 15;
    unsigned offR[2];
#pragma unroll
    for (int kk = 0; kk < 2; ++kk) offR[kk] = rr * 128 + (unsigned)(((4 * kk + q4) ^ ((rr >> 1) & 7)) << 4);
    auto ldf = [&](unsigned a, int imm) { return *(const LAS bf16x8*)(LAS char*)(size_t)(a + imm); };
    f32x4 acc1[4][4] = {};
    bf16x8 ga[2][2][4], gb[2][2][4];
    unsigned aA[2], aB[3][2];
#pragma unroll
    for (int h = 0; h < 3; ++h) {
        if (h < 2) { aA[h] = h * 4 * UNIT + wm * 8192 + lane * 16; asm volatile("" : "+v"(aA[h])); }
        aB[h][0] = h * 4 * UNIT + wn * 8192 + offR[0];
        aB[h][1] = h * 4 * UNIT + wn * 8192 + offR[1];
        asm volatile("" : "+v"(aB[h][0]), "+v"(aB[h][1]));
    }
    auto rd1 = [&](int set, int i1, int j0, int j1) {
        const int ra = regA(i1), rb = regB(i1);
#pragma unroll
        for (int kk = 0; kk < 2; ++kk)
#pragma unroll
            for (int i = 0; i < 4; ++i) {
                if (i >= j0 && i < j1) gb[set][kk][i] = ldf(aB[rb >> 2][kk], (rb & 3) * UNIT + i * 2048);
                ga[set][kk][i] = ldf(aA[ra >> 2], (ra & 3) * UNIT + kk * 1024 + i * 2048);
            }
    };
    {
        f32x4 acc[2][4][2] = {};
        bf16x8 fa[3][2][4], fb[3][2][2];
        unsigned ax[2] = {XR_OFF + offR[0], XR_OFF + offR[1]};
        unsigned ab[2][2] = {{wid * 4096 + offR[0], wid * 4096 + offR[1]},
                             {4 * UNIT + wid * 4096 + offR[0], 4 * UNIT + wid * 4096 + offR[1]}};
        asm volatile("" : "+v"(ax[0]), "+v"(ax[1]), "+v"(ab[0][0]), "+v"(ab[0][1]), "+v"(ab[1][0]), "+v"(ab[1][1]));
        auto bset = [](int q) { return (q < 2 * K1 ? (q >> 1) : q - K1) % 3; };
        auto rd = [&](int q) {
            const int un = qfill(q) % 6;
#pragma unroll
            for (int kk = 0; kk < 2; ++kk) {
                if (q >= 2 * K1 || !(q & 1)) {
#pragma unroll
                    for (int i = 0; i < 2; ++i) fb[bset(q)][kk][i] = ldf(ab[un >> 2][kk], (un & 3) * UNIT + i * 2048);
                }
#pragma unroll
                for (int i = 0; i < 4; ++i) fa[q % 3][kk][i] = ldf(ax[kk], (q & 7) * 8192 + i * 2048);
            }
        };
        auto mm = [&](int q) {
#pragma unroll
            for (int kk = 0; kk < 2; ++kk)
#pragma unroll
                for (int i = 0; i < 4; ++i)
#pragma unroll
                    for (int jj = 0; jj < 2; ++jj)
                        acc[qhalf(q)][i][jj] = __builtin_amdgcn_mfma_f32_16x16x32_bf16(fa[q % 3][kk][i], fb[bset(q)][kk][jj],
                                                                                      acc[qhalf(q)][i][jj], 0, 0, 0);
        };
        const __amdgpu_buffer_rsrc_t ar =
            make_rsrc(at + ((size_t)((z * 8 + te) * 32 + tm) * 16 + wid * 4) * 512, 16 * 16384 + 4096);
        auto publish = [&](int half) {
            u32x4 pk[4];
#pragma unroll
            for (int c = 0; c < 4; ++c) {
                pk[c] = pack8(acc[half][2 * (c & 1)][c >> 1], acc[half][2 * (c & 1) + 1][c >> 1]) ^ (z ? 0x80008000u : 0u);
                if (half) *(LAS u32x4*)(smem + regA(1) * UNIT + wid * 4096 + c * 1024 + lane * 16) = pk[c];
            }
#pragma unroll
            for (int c = 0; c < 4; ++c)
                __builtin_amdgcn_raw_buffer_store_b128(pk[c], ar, lane * 16 + c * 1024, half * 16 * 16384, SC1);
            __builtin_amdgcn_sched_barrier(0);
        };
        bar_raw();
        asm volatile("s_waitcnt vmcnt(0)" : "+v"(sink)::"memory");
        rd(0);
        rd(1);
        bar_lgkm();
#pragma unroll
        for (int n = 0; n < 16; ++n) {
            if (n < 15) rd(2 * n + 2); else rd1(0, 0, 0, 4);
            mm(2 * n);
            interleave(n < 15 ? 12 : 16);
            if (n < 15) rd(2 * n + 3);
            mm(2 * n + 1);
            interleave(n < K1 - 1 ? 8 : n < 15 ? 12 : 0);
            if (n == DRA) wait_vm<0>();
            if (n == NA - 1) publish(0);
            if (n == 15) publish(1);
            bar_lgkm();
        }
    }
    auto mm1 = [&](int set, int j0, int j1) {
#pragma unroll
        for (int kk = 0; kk < 2; ++kk)
#pragma unroll
            for (int i = 0; i < 4; ++i)
#pragma unroll
                for (int jj = j0; jj < j1; ++jj)
                    acc1[i][jj] = __builtin_amdgcn_mfma_f32_16x16x32_bf16(ga[set][kk][i], gb[set][kk][jj], acc1[i][jj], 0, 0, 0);
    };
#pragma unroll
    for (int i1 = 0; i1 < 28; ++i1) {
        rd1((i1 + 1) & 1, i1 + 1, i1 < 27 ? 0 : 2, 4);
        mm1(i1 & 1, 0, 4);
        interleave(i1 < 27 ? 16 : 12);
        if (i1 + 16 == DRB) wait_vm<0>();
        bar_lgkm();
    }
    const __amdgpu_buffer_rsrc_t yr = make_rsrc(y + (size_t)(tm * 128 + wn * 64) * HID + te * 128 + wm * 64, 64 * HID * 2);
    const unsigned box = (unsigned)(lane >> 3) * (HID * 2) + (lane & 7) * 16;
    u32x4 oth[4];
#pragma unroll
    for (int h = 0; h < 8; ++h) {
        if (h == 7) {
#pragma unroll
            for (int c = 0; c < 4; ++c) oth[c] = __builtin_amdgcn_raw_buffer_load_b128(yr, box, (z * 32 + c * 8) * HID * 2, SC1);
            __builtin_amdgcn_sched_barrier(0);
        }
        if (h < 7) rd1((h + 1) & 1, 28 + ((h + 1) & 3), h < 3 ? 2 : 0, h < 3 ? 4 : 2);
        mm1(h & 1, h < 4 ? 2 : 0, h < 4 ? 4 : 2);
        interleave(h < 7 ? 12 : 0);
        if (h == 1) bar_lgkm();
        if (h == 3) {
#pragma unroll
            for (int c = 0; c < 4; ++c) {
                u32x4 v = pack8(acc1[2 * (c & 1)][2 + (c >> 1)], acc1[2 * (c & 1) + 1][2 + (c >> 1)]);
#pragma unroll
                for (int e = 0; e < 4; ++e) v[e] = v[e] < (SENT - 1u) ? v[e] : (SENT - 1u);
                asm volatile("" : "+v"(v));
                __builtin_amdgcn_raw_buffer_store_b128(v, yr, box, ((z ^ 1) * 32 + c * 8) * HID * 2, SC1);
                asm volatile("s_nop 1" ::"v"(v));
            }
            __builtin_amdgcn_sched_barrier(0);
        }
    }
    f32x2 own[4][4];
#pragma unroll
    for (int c = 0; c < 4; ++c) {
        const u32x4 v = pack8(acc1[2 * (c & 1)][c >> 1], acc1[2 * (c & 1) + 1][c >> 1]);
#pragma unroll
        for (int e = 0; e < 4; ++e) {
            own[c][e] = (f32x2){__builtin_bit_cast(float, v[e] << 16), __builtin_bit_cast(float, v[e] & 0xFFFF0000u)};
            asm volatile("" : "+v"(own[c][e]));
        }
    }
#pragma unroll 1
    for (unsigned spin = 0; spin < SPIN_CAP; ++spin) {
        unsigned mx = 0;
#pragma unroll
        for (int c = 0; c < 4; ++c)
#pragma unroll
            for (int e = 0; e < 4; ++e) mx = mx > oth[c][e] ? mx : oth[c][e];
        if (__builtin_expect(__all(mx != SENT), 1)) break;
        cfence();
#pragma unroll
        for (int c = 0; c < 4; ++c) oth[c] = __builtin_amdgcn_raw_buffer_load_b128(yr, box, (z * 32 + c * 8) * HID * 2, SC1);
    }
    cfence();
    const unsigned yo = (unsigned)rr * (HID * 2) + q4 * 16;
#pragma unroll
    for (int c = 0; c < 4; ++c) {
        u32x4 o;
#pragma unroll
        for (int e = 0; e < 4; ++e) {
            const f32x2 p = {__builtin_bit_cast(float, oth[c][e] << 16), __builtin_bit_cast(float, oth[c][e] & 0xFFFF0000u)};
            const f32x2 r = own[c][e] + p;
            o[e] = pack_bf16(r[0], r[1]);
        }
        __builtin_amdgcn_raw_buffer_store_b128(o, yr, yo, (z * 32 + (c >> 1) * 16) * HID * 2 + (c & 1) * 64, SC1);
    }
}

extern "C" void kernel_launch(void* const* d_in, const int* in_sizes, int n_in, void* d_out, int out_size, void* d_ws,
                              size_t ws_size, hipStream_t stream) {
    const u16* x = (const u16*)d_in[0];
    const u16* sre = (const u16*)d_in[1];
    const u16* sim = (const u16*)d_in[2];
    const u16* wre = (const u16*)d_in[3];
    const u16* wim = (const u16*)d_in[4];
    u16* at = (u16*)d_ws;
    u64* ctl = (u64*)((char*)d_ws + AT_BYTES);
    (void)hipFuncSetAttribute((const void*)fnet_fused, hipFuncAttributeMaxDynamicSharedMemorySize, SHM);
    fnet_fused<<<256, 512, SHM, stream>>>(x, sre, sim, wre, wim, at, ctl, (u16*)d_out);
}
```

```cpp
#include <hip/hip_runtime.h>
#include <hip/hip_bf16.h>
#include <stdint.h>

typedef unsigned short u16;
typedef unsigned long long u64;
typedef short bf16x8 __attribute__((ext_vector_type(8)));
typedef float f32x4 __attribute__((ext_vector_type(4)));
typedef float f32x2 __attribute__((ext_vector_type(2)));
typedef __bf16 bf16x2_t __attribute__((ext_vector_type(2)));
typedef unsigned u32x4 __attribute__((ext_vector_type(4)));
#define LAS __attribute__((address_space(3)))
#define GAS __attribute__((address_space(1)))

constexpr int SEQ = 2048, HID = 1024;
constexpr int UNIT = 16384;
constexpr int XR_OFF = 6 * UNIT;
constexpr int SHM = 10 * UNIT;
constexpr size_t AT_BYTES = 2ull * HID * SEQ * 2;
constexpr int CTL_U64 = 128;
constexpr unsigned SENT = 0xFFFFFFFFu;
constexpr unsigned SPIN_CAP = 1u << 18;
constexpr int SC1 = 16;

constexpr int K1 = 6;
constexpr int QB = 16 + K1;
constexpr int NA = QB / 2;
constexpr int NW = 26 - K1;
constexpr int qhalf(int q) { return q < 2 * K1 ? (q & 1) : (q >= QB); }
constexpr int qfill(int q) { return q < 2 * K1 ? (q >> 1) : (q < QB ? q - K1 : q - QB + 10); }
constexpr int fillk(int f) { return f < 16 ? f : f - 16 + K1; }
constexpr int qk(int q) { return fillk(qfill(q)); }
constexpr int regA(int i) { return (i & 3) == 0 ? 6 : qfill((i & 3) == 1 ? 29 : (i & 3) == 2 ? 28 : 30) % 6; }
constexpr int regB(int i) { return (i & 3) < 2 ? qfill(26 + (i & 3)) % 6 : 5 + (i & 3); }
constexpr int firstq(int f) { for (int q = 0; q < 32; ++q) if (qfill(q) == f) return q; return 0; }
constexpr int lastq(int f) { for (int q = 31; q >= 0; --q) if (qfill(q) == f && (q >= 2 * K1 || !(q & 1))) return q; return 0; }
constexpr int bdead(int f) { return f < NW ? firstq(f) / 2 - 2 : 14 + (f - NW) - (f - NW >= 30); }
constexpr int bearly(int f) {
    return f < 6 ? -2 : f < NW ? lastq(f - 6) / 2 : f - NW < 3 ? 13 : f - NW == 3 ? 14 : 12 + (f - NW);
}
struct BSched {
    int issue[64], dead[64], allow[64];
    bool ok;
    constexpr BSched() : issue{}, dead{}, allow{}, ok(true) {
        int p1 = -9, p2 = -9;
        for (int g = 0; g < NW + 32; ++g) {
            const int e = bearly(g);
            dead[g] = bdead(g);
            int v = g < NW ? dead[g] - 3 : e;
            if (v < e) v = e;
            if (v < g / 2 - 2) v = g / 2 - 2;
            if (v < p1) v = p1;
            if (v == p2) ++v;
            p2 = p1; p1 = v;
            issue[g] = v;
            if (v > dead[g] || (g && dead[g] < dead[g - 1])) ok = false;
        }
        for (int n = -2; n < 46; ++n) {
            int c = 0;
            for (int f = 0; f < NW + 32; ++f) if (issue[f] <= n && dead[f] > n) ++c;
            allow[n + 2] = c;
            if (c > 5) ok = false;
        }
    }
};
constexpr BSched kB{};
static_assert(kB.ok, "loader schedule");
constexpr int FILL0 = 2;
constexpr int DRA = NA, DRB = 21;
constexpr int CKA = 14, CKA2 = 16, CKA3 = 20, CKB = 28;
static_assert(FILL0 + 8 <= CKA && CKA <= DRB, "boxes are drained before the hi flag");
static_assert(DRA <= 12, "own lo slice is loaded in interval 13");
constexpr int wfind(int n) {
    constexpr int d[16] = {7, 8, 9, 12, 15, 17, 18, 21, 23, 25, 27, 29, 31, 33, 35, 37};
    for (int t = 0; t < 16; ++t) if (d[t] == n) return t;
    return -1;
}
__device__ __forceinline__ int kappa(int i, int tm) {
    return i == 0 ? tm : i == 1 ? 16 + tm : i <= 16 ? (tm ^ (i - 1)) : 16 + (tm ^ (i - 16));
}
template <int I> struct IC { static constexpr int v = I; };
template <int I, int N, class F> __device__ __forceinline__ void static_for(F&& f) {
    if constexpr (I < N) { f(IC<I>{}); static_for<I + 1, N>(f); }
}

template <int N> __device__ __forceinline__ void wait_vm() { asm volatile("s_waitcnt vmcnt(%0)" ::"n"(N) : "memory"); }
__device__ __forceinline__ void bar_raw() { asm volatile("s_barrier" ::: "memory"); }
__device__ __forceinline__ void bar_lgkm() {
    __builtin_amdgcn_sched_barrier(0);
    __builtin_amdgcn_s_waitcnt(0xC07F);
    asm volatile("s_barrier" ::: "memory");
    __builtin_amdgcn_sched_barrier(0);
}
__device__ __forceinline__ void cfence() { asm volatile("" ::: "memory"); }
__device__ __forceinline__ unsigned pack_bf16(float lo, float hi) {
    f32x2 v = {lo, hi};
    bf16x2_t r = __builtin_convertvector(v, bf16x2_t);
    return __builtin_bit_cast(unsigned, r);
}
__device__ __forceinline__ u32x4 pack8(f32x4 a, f32x4 b) {
    u32x4 r = {pack_bf16(a[0], a[1]), pack_bf16(a[2], a[3]), pack_bf16(b[0], b[1]), pack_bf16(b[2], b[3])};
    return r;
}
__device__ __forceinline__ __amdgpu_buffer_rsrc_t make_rsrc(const void* p, unsigned bytes) {
    return __builtin_amdgcn_make_buffer_rsrc((void*)p, 0, bytes, 0x00020000);
}
__device__ __forceinline__ void touch64(unsigned& sink, const void* p) {
    asm volatile("global_load_dword %0, %1, off" : "+v"(sink) : "v"(p) : "memory");
}
__device__ __forceinline__ void interleave(int reads) {
#pragma unroll
    for (int g = 0; g < 16; ++g) {
        if (g < reads) {
            __builtin_amdgcn_sched_group_barrier(0x008, 1, 0);
            __builtin_amdgcn_sched_group_barrier(0x100, 1, 0);
        }
    }
    __builtin_amdgcn_sched_group_barrier(0x008, 16, 0);
    __builtin_amdgcn_sched_barrier(0);
}

template <bool PERM>
__device__ __forceinline__ void row_offsets(unsigned (&vo)[8], int lane, unsigned ldb, int rx) {
    const int r = lane >> 3, c = lane & 7;
#pragma unroll
    for (int i = 0; i < 8; ++i) {
        const int row = PERM ? 32 * (i >> 2) + 16 * (i & 1) + 4 * ((i >> 1) & 1) + 8 * (r >> 2) + (r & 3) : (8 * i + r) ^ rx;
        vo[i] = row * ldb + ((c ^ (4 * (i & 1) + (r >> 1))) << 4) - (i & 3) * 1024;
        asm volatile("" : "+v"(vo[i]));
    }
}
__device__ __forceinline__ void issue_rows(LAS char* dst, __amdgpu_buffer_rsrc_t rsrc, const unsigned (&vo)[8], unsigned so) {
#pragma unroll
    for (int h = 0; h < 2; ++h) {
        __builtin_amdgcn_raw_ptr_buffer_load_lds(rsrc, (LAS void*)(dst + h * 4096), 16, vo[4 * h + 0], so, 0, 0);
        __builtin_amdgcn_raw_ptr_buffer_load_lds(rsrc, (LAS void*)(dst + h * 4096), 16, vo[4 * h + 1], so, 1024, 0);
        __builtin_amdgcn_raw_ptr_buffer_load_lds(rsrc, (LAS void*)(dst + h * 4096), 16, vo[4 * h + 2], so, 2048, 0);
        __builtin_amdgcn_raw_ptr_buffer_load_lds(rsrc, (LAS void*)(dst + h * 4096), 16, vo[4 * h + 3], so, 3072, 0);
    }
}
__device__ __forceinline__ void issue_at(LAS char* dst, __amdgpu_buffer_rsrc_t rsrc, const unsigned (&vo)[4], unsigned so) {
#pragma unroll
    for (int i = 0; i < 4; ++i) {
        __builtin_amdgcn_raw_ptr_buffer_load_lds(rsrc, (LAS void*)(dst + i * 4096), 16, vo[i], so, 0, SC1);
        __builtin_amdgcn_raw_ptr_buffer_load_lds(rsrc, (LAS void*)(dst + i * 4096), 16, vo[i], so, 1024, SC1);
        __builtin_amdgcn_raw_ptr_buffer_load_lds(rsrc, (LAS void*)(dst + i * 4096), 16, vo[i], so, 2048, SC1);
        __builtin_amdgcn_raw_ptr_buffer_load_lds(rsrc, (LAS void*)(dst + i * 4096), 16, vo[i], so, 3072, SC1);
    }
}

__global__ __launch_bounds__(512, 2) void fnet_fused(const u16* __restrict__ x, const u16* __restrict__ sre,
                                                     const u16* __restrict__ sim, const u16* __restrict__ wre,
                                                     const u16* __restrict__ wim, u16* at, u64* ctl, u16* y) {
    extern __shared__ __attribute__((aligned(16))) char smem_g[];
    LAS char* smem = (LAS char*)smem_g;
    const int b = blockIdx.x, xcd = b & 7, j = b >> 3;
    const int tm = (xcd & 3) * 4 + (j & 3);
    const int z = xcd >> 2, te = j >> 2;
    const int wid = __builtin_amdgcn_readfirstlane(threadIdx.x >> 6), lane = threadIdx.x & 63;
    const int k0 = 4 * (xcd & 3) + 8 * z;
    unsigned sink = 0;
    if (wid < 4) {
        const u16* src = wid < 2 ? (z ? wim : wre) + (size_t)(32 * j + 16 * wid + (lane >> 2)) * HID + ((k0 + (lane & 3)) & 15) * 64
                                 : x + (size_t)((wid & 1) * 1024 + (xcd & 3) * 256 + 8 * j + (lane >> 3)) * HID + ((k0 + (lane & 7)) & 15) * 64;
        touch64(sink, src);
    }
    if (wid == 0 && j < 4) {
        unsigned long long pc0;
        asm volatile("s_getpc_b64 %0" : "=s"(pc0));
        touch64(sink, (const char*)pc0 + 4096 + j * 8192 + lane * 128);
    }
    const u16* ws = (z ? sim : sre) + (size_t)tm * 128 * SEQ;

    if (wid == 5) {
        GAS u64* grp = (GAS u64*)ctl + te * CTL_U64;
        u64 tag = __hip_atomic_load(grp + 64, __ATOMIC_RELAXED, __HIP_MEMORY_SCOPE_AGENT) + 1;
        const char* pw = (const char*)(ws + (size_t)(lane >> 1) * SEQ);
        const char* pw0 = pw + kappa(2 * te + (lane & 1), tm) * 128;
        const char* pw1 = pw + kappa(16 + 2 * te + (lane & 1), tm) * 128;
        const __amdgpu_buffer_rsrc_t br = make_rsrc(y + (size_t)(tm * 128 + z * 32) * HID + te * 128, 96 * HID * 2);
        const unsigned bo = (unsigned)(lane >> 4) * (HID * 2) + (lane & 15) * 16;
        const u32x4 s4 = {SENT, SENT, SENT, SENT};
        bar_raw();
        bar_raw();
        asm volatile("" : "+v"(tag));
        u64 pv = 0;
        static_for<0, 45>([&](auto N) {
            constexpr int n = decltype(N)::v, rnd = n > 15, t = wfind(n);
            if constexpr (n == DRA + 1 || n == DRB + 1) {
                if (lane == 0)
                    __hip_atomic_store(grp + rnd * 32 + z * 16 + tm, tag, __ATOMIC_RELAXED, __HIP_MEMORY_SCOPE_AGENT);
            }
            if constexpr (n >= FILL0 && n < FILL0 + 8) {
                constexpr int row = ((n - FILL0) >> 2) * 64 + ((n - FILL0) & 3) * 8;
                __builtin_amdgcn_raw_buffer_store_b128(s4, br, bo, row * HID * 2, SC1);
                __builtin_amdgcn_raw_buffer_store_b128(s4, br, bo, (row + 4) * HID * 2, SC1);
            }
            if constexpr (t >= 0) {
                if (__builtin_expect((t & 7) == te, 0)) {
                    const char* p = t >= 8 ? pw1 : pw0;
#pragma unroll
                    for (int i = 0; i < 4; ++i) touch64(sink, p + (size_t)i * 32 * SEQ * 2);
                }
            }
            constexpr bool hi = n > CKA3;
            constexpr int lim = n <= CKA ? 4 : n <= CKA2 ? 8 : 16;
            GAS u64* f = grp + (hi ? 32 + (lane & 31) : z * 16 + (tm ^ (lane & (lim - 1))));
            if constexpr (n == CKA - 1 || n == CKA2 - 1 || n == CKA3 - 1 || n == CKB - 2)
                pv = __hip_atomic_load(f, __ATOMIC_RELAXED, __HIP_MEMORY_SCOPE_AGENT);
            if constexpr (n == CKA || n == CKA2 || n == CKA3 || n == CKB) {
                if constexpr (n == CKA) wait_vm<0>();
                if (__builtin_expect(!__all(pv == tag), 0)) {
#pragma unroll 1
                    for (unsigned spin = 0; spin < SPIN_CAP; ++spin) {
                        const u64 v = __hip_atomic_load(f, __ATOMIC_RELAXED, __HIP_MEMORY_SCOPE_AGENT);
                        if (__all(v == tag)) break;
                        __builtin_amdgcn_s_sleep(1);
                    }
                }
                if (hi && b == te * 32 && lane == 0)
                    __hip_atomic_store(grp + 64, tag, __ATOMIC_RELAXED, __HIP_MEMORY_SCOPE_AGENT);
            }
            bar_raw();
        });
        asm volatile("s_waitcnt vmcnt(0)" : "+v"(sink)::"memory");
        return;
    }
    if (wid >= 4) {
        unsigned vo[8];
        row_offsets<true>(vo, lane, HID * 2, 0);
        if (wid == 4) {
            const __amdgpu_buffer_rsrc_t r0 = make_rsrc(x + (size_t)tm * 64 * HID, (1024 + 64) * HID * 2);
            const __amdgpu_buffer_rsrc_t r1 = make_rsrc(at + (size_t)(z * 8 + te) * 32 * 8192, 32 * 16384);
            unsigned va[4];
#pragma unroll
            for (int i = 0; i < 4; ++i) { va[i] = lane * 16 + i * 4096; asm volatile("" : "+v"(va[i])); }
            auto ldx = [&](int q) {
                issue_rows(smem + XR_OFF + (q & 7) * 8192, r0, vo, qhalf(q) * 1024 * HID * 2 + ((qk(q) + k0) & 15) * 128);
            };
            auto lda = [&](int i) { issue_at(smem + regA(i) * UNIT, r1, va, kappa(i, tm) * 16384); };
#pragma unroll
            for (int q = 0; q < 4; ++q) ldx(q);
            wait_vm<16>(); bar_raw();
#pragma unroll
            for (int q = 4; q < 8; ++q) ldx(q);
            wait_vm<32>(); bar_raw();
#pragma unroll
            for (int n = 0; n < 12; ++n) { ldx(2 * n + 8); ldx(2 * n + 9); wait_vm<32>(); bar_raw(); }
            wait_vm<16>(); bar_raw();
            lda(0); wait_vm<16>(); bar_raw();
            wait_vm<0>(); bar_raw();
            lda(2); lda(3); bar_raw();
            lda(4); wait_vm<32>(); bar_raw();
#pragma unroll
            for (int i = 5; i < 31; ++i) { lda(i); wait_vm<32>(); bar_raw(); }
            lda(31); wait_vm<16>(); bar_raw();
            wait_vm<0>(); bar_raw();
        } else {
            const int hf = wid & 1;
            const __amdgpu_buffer_rsrc_t r0 = make_rsrc((z ? wim : wre) + (size_t)(te * 128 + hf * 64) * HID, 64 * HID * 2);
            const __amdgpu_buffer_rsrc_t r1 = make_rsrc(ws + (size_t)hf * 64 * SEQ, 64 * SEQ * 2);
            unsigned vw[8];
            row_offsets<false>(vw, lane, SEQ * 2, z * 32);
            LAS char* dst = smem + hf * 8192;
            static_for<0, 47>([&](auto N) {
                constexpr int n = decltype(N)::v - 2;
                static_for<0, NW + 32>([&](auto F) {
                    constexpr int f = decltype(F)::v;
                    if constexpr (kB.issue[f] == n) {
                        if constexpr (f < NW) issue_rows(dst + (f % 6) * UNIT, r0, vo, ((fillk(f) + k0) & 15) * 128);
                        else issue_rows(dst + regB(f - NW) * UNIT, r1, vw, kappa(f - NW, tm) * 128);
                    }
                });
                wait_vm<8 * kB.allow[n + 2]>();
                bar_raw();
            });
        }
        return;
    }

    const int wm = wid >> 1, wn = wid & 1;
    const int q4 = lane >> 4, rr = lane & 15;
    unsigned offR[2];
#pragma unroll
    for (int kk = 0; kk < 2; ++kk) offR[kk] = rr * 128 + (unsigned)(((4 * kk + q4) ^ ((rr >> 1) & 7)) << 4);
    auto ldf = [&](unsigned a, int imm) { return *(const LAS bf16x8*)(LAS char*)(size_t)(a + imm); };
    f32x4 acc1[4][4] = {};
    bf16x8 ga[2][2][4], gb[2][2][4];
    unsigned aA[2], aB[3][2];
#pragma unroll
    for (int h = 0; h < 3; ++h) {
        if (h < 2) { aA[h] = h * 4 * UNIT + wm * 8192 + lane * 16; asm volatile("" : "+v"(aA[h])); }
        aB[h][0] = h * 4 * UNIT + wn * 8192 + offR[0];
        aB[h][1] = h * 4 * UNIT + wn * 8192 + offR[1];
        asm volatile("" : "+v"(aB[h][0]), "+v"(aB[h][1]));
    }
    auto rd1 = [&](int set, int i1, int j0, int j1) {
        const int ra = regA(i1), rb = regB(i1);
#pragma unroll
        for (int kk = 0; kk < 2; ++kk)
#pragma unroll
            for (int i = 0; i < 4; ++i) {
                if (i >= j0 && i < j1) gb[set][kk][i] = ldf(aB[rb >> 2][kk], (rb & 3) * UNIT + i * 2048);
                ga[set][kk][i] = ldf(aA[ra >> 2], (ra & 3) * UNIT + kk * 1024 + i * 2048);
            }
    };
    {
        f32x4 acc[2][4][2] = {};
        bf16x8 fa[3][2][4], fb[3][2][2];
        unsigned ax[2] = {XR_OFF + offR[0], XR_OFF + offR[1]};
        unsigned ab[2][2] = {{wid * 4096 + offR[0], wid * 4096 + offR[1]},
                             {4 * UNIT + wid * 4096 + offR[0], 4 * UNIT + wid * 4096 + offR[1]}};
        asm volatile("" : "+v"(ax[0]), "+v"(ax[1]), "+v"(ab[0][0]), "+v"(ab[0][1]), "+v"(ab[1][0]), "+v"(ab[1][1]));
        auto bset = [](int q) { return (q < 2 * K1 ? (q >> 1) : q - K1) % 3; };
        auto rd = [&](int q) {
            const int un = qfill(q) % 6;
#pragma unroll
            for (int kk = 0; kk < 2; ++kk) {
                if (q >= 2 * K1 || !(q & 1)) {
#pragma unroll
                    for (int i = 0; i < 2; ++i) fb[bset(q)][kk][i] = ldf(ab[un >> 2][kk], (un & 3) * UNIT + i * 2048);
                }
#pragma unroll
                for (int i = 0; i < 4; ++i) fa[q % 3][kk][i] = ldf(ax[kk], (q & 7) * 8192 + i * 2048);
            }
        };
        auto mm = [&](int q) {
#pragma unroll
            for (int kk = 0; kk < 2; ++kk)
#pragma unroll
                for (int i = 0; i < 4; ++i)
#pragma unroll
                    for (int jj = 0; jj < 2; ++jj)
                        acc[qhalf(q)][i][jj] = __builtin_amdgcn_mfma_f32_16x16x32_bf16(fa[q % 3][kk][i], fb[bset(q)][kk][jj],
                                                                                      acc[qhalf(q)][i][jj], 0, 0, 0);
        };
        const __amdgpu_buffer_rsrc_t ar =
            make_rsrc(at + ((size_t)((z * 8 + te) * 32 + tm) * 16 + wid * 4) * 512, 16 * 16384 + 4096);
        auto publish = [&](int half) {
            u32x4 pk[4];
#pragma unroll
            for (int c = 0; c < 4; ++c) {
                pk[c] = pack8(acc[half][2 * (c & 1)][c >> 1], acc[half][2 * (c & 1) + 1][c >> 1]) ^ (z ? 0x80008000u : 0u);
                if (half) *(LAS u32x4*)(smem + regA(1) * UNIT + wid * 4096 + c * 1024 + lane * 16) = pk[c];
            }
#pragma unroll
            for (int c = 0; c < 4; ++c)
                __builtin_amdgcn_raw_buffer_store_b128(pk[c], ar, lane * 16 + c * 1024, half * 16 * 16384, SC1);
            __builtin_amdgcn_sched_barrier(0);
        };
        bar_raw();
        asm volatile("s_waitcnt vmcnt(0)" : "+v"(sink)::"memory");
        rd(0);
        rd(1);
        bar_lgkm();
#pragma unroll
        for (int n = 0; n < 16; ++n) {
            if (n < 15) rd(2 * n + 2); else rd1(0, 0, 0, 4);
            mm(2 * n);
            interleave(n < 15 ? 12 : 16);
            if (n < 15) rd(2 * n + 3);
            mm(2 * n + 1);
            interleave(n < K1 - 1 ? 8 : n < 15 ? 12 : 0);
            if (n == DRA) wait_vm<0>();
            if (n == NA - 1) publish(0);
            if (n == 15) publish(1);
            bar_lgkm();
        }
    }
    auto mm1 = [&](int set, int j0, int j1) {
#pragma unroll
        for (int kk = 0; kk < 2; ++kk)
#pragma unroll
            for (int i = 0; i < 4; ++i)
#pragma unroll
                for (int jj = j0; jj < j1; ++jj)
                    acc1[i][jj] = __builtin_amdgcn_mfma_f32_16x16x32_bf16(ga[set][kk][i], gb[set][kk][jj], acc1[i][jj], 0, 0, 0);
    };
#pragma unroll
    for (int i1 = 0; i1 < 28; ++i1) {
        rd1((i1 + 1) & 1, i1 + 1, i1 < 27 ? 0 : 2, 4);
        mm1(i1 & 1, 0, 4);
        interleave(i1 < 27 ? 16 : 12);
        if (i1 + 16 == DRB) wait_vm<0>();
        bar_lgkm();
    }
    const __amdgpu_buffer_rsrc_t yr = make_rsrc(y + (size_t)(tm * 128 + wn * 64) * HID + te * 128 + wm * 64, 64 * HID * 2);
    const unsigned box = (unsigned)(lane >> 3) * (HID * 2) + (lane & 7) * 16;
    u32x4 oth[4];
#pragma unroll
    for (int h = 0; h < 8; ++h) {
        if (h == 7) {
#pragma unroll
            for (int c = 0; c < 4; ++c) oth[c] = __builtin_amdgcn_raw_buffer_load_b128(yr, box, (z * 32 + c * 8) * HID * 2, SC1);
            __builtin_amdgcn_sched_barrier(0);
        }
        if (h < 7) rd1((h + 1) & 1, 28 + ((h + 1) & 3), h < 3 ? 2 : 0, h < 3 ? 4 : 2);
        mm1(h & 1, h < 4 ? 2 : 0, h < 4 ? 4 : 2);
        interleave(h < 7 ? 12 : 0);
        if (h == 1) bar_lgkm();
        if (h == 3) {
#pragma unroll
            for (int c = 0; c < 4; ++c) {
                u32x4 v = pack8(acc1[2 * (c & 1)][2 + (c >> 1)], acc1[2 * (c & 1) + 1][2 + (c >> 1)]);
#pragma unroll
                for (int e = 0; e < 4; ++e) v[e] = v[e] < (SENT - 1u) ? v[e] : (SENT - 1u);
                asm volatile("" : "+v"(v));
                __builtin_amdgcn_raw_buffer_store_b128(v, yr, box, ((z ^ 1) * 32 + c * 8) * HID * 2, SC1);
                asm volatile("s_nop 1" ::"v"(v));
            }
            __builtin_amdgcn_sched_barrier(0);
        }
    }
    f32x2 own[4][4];
#pragma unroll
    for (int c = 0; c < 4; ++c) {
        const u32x4 v = pack8(acc1[2 * (c & 1)][c >> 1], acc1[2 * (c & 1) + 1][c >> 1]);
#pragma unroll
        for (int e = 0; e < 4; ++e) {
            own[c][e] = (f32x2){__builtin_bit_cast(float, v[e] << 16), __builtin_bit_cast(float, v[e] & 0xFFFF0000u)};
            asm volatile("" : "+v"(own[c][e]));
        }
    }
#pragma unroll 1
    for (unsigned spin = 0; spin < SPIN_CAP; ++spin) {
        unsigned mx = 0;
#pragma unroll
        for (int c = 0; c < 4; ++c)
#pragma unroll
            for (int e = 0; e < 4; ++e) mx = mx > oth[c][e] ? mx : oth[c][e];
        if (__builtin_expect(__all(mx != SENT), 1)) break;
        cfence();
#pragma unroll
        for (int c = 0; c < 4; ++c) oth[c] = __builtin_amdgcn_raw_buffer_load_b128(yr, box, (z * 32 + c * 8) * HID * 2, SC1);
    }
    cfence();
    const unsigned yo = (unsigned)rr * (HID * 2) + q4 * 16;
#pragma unroll
    for (int c = 0; c < 4; ++c) {
        u32x4 o;
#pragma unroll
        for (int e = 0; e < 4; ++e) {
            const f32x2 p = {__builtin_bit_cast(float, oth[c][e] << 16), __builtin_bit_cast(float, oth[c][e] & 0xFFFF0000u)};
            const f32x2 r = own[c][e] + p;
            o[e] = pack_bf16(r[0], r[1]);
        }
        __builtin_amdgcn_raw_buffer_store_b128(o, yr, yo, (z * 32 + (c >> 1) * 16) * HID * 2 + (c & 1) * 64, SC1);
    }
}

extern "C" void kernel_launch(void* const* d_in, const int* in_sizes, int n_in, void* d_out, int out_size, void* d_ws,
                              size_t ws_size, hipStream_t stream) {
    const u16* x = (const u16*)d_in[0];
    const u16* sre = (const u16*)d_in[1];
    const u16* sim = (const u16*)d_in[2];
    const u16* wre = (const u16*)d_in[3];
    const u16* wim = (const u16*)d_in[4];
    u16* at = (u16*)d_ws;
    u64* ctl = (u64*)((char*)d_ws + AT_BYTES);
    (void)hipFuncSetAttribute((const void*)fnet_fused, hipFuncAttributeMaxDynamicSharedMemorySize, SHM);
    fnet_fused<<<256, 512, SHM, stream>>>(x, sre, sim, wre, wim, at, ctl, (u16*)d_out);
}
```
